```python
import torch

def _softmax(x, axis=-1):
    return torch.softmax(x, dim=axis)
def _gelu(x):
    return torch.nn.functional.gelu(x, approximate="tanh")
def _silu(x):
    return torch.nn.functional.silu(x)
def _sigmoid(x):
    return torch.sigmoid(x)
def _layernorm(x, axis=-1, eps=1e-5):
    m = x.mean(dim=axis, keepdim=True)
    v = x.var(dim=axis, keepdim=True, unbiased=False)
    return (x - m) / torch.sqrt(v + eps)
def _scatter_add(tgt, idx, src, axis=0):
    return tgt.clone().index_add_(axis, idx, src)
def _segment_sum(src, seg, n):
    out = torch.zeros((n,) + src.shape[1:], dtype=src.dtype)
    return out.index_add_(0, seg, src)
def _pad(x, pw):
    flat = tuple(v for pair in reversed(pw) for v in pair)
    return torch.nn.functional.pad(x, flat)

kernel_name = 'sequential_preLN__sliding_block__dense_activation_relu__prompt'

def setup_inputs():

    _w = (lambda a: (lambda t: t.to(torch.bfloat16) if t.is_floating_point() else t)(torch.as_tensor(a)))
    def _A_setup():
        B, S, D = 1, 2048, 448
        return {
            "x": _w((lambda *s: torch.randn(*s, dtype=torch.bfloat16) * 0.05)(B, S, D)),
            "wqkv": _w((lambda *s: torch.randn(*s, dtype=torch.bfloat16) * 0.05)(D, 3 * D)),
            "wo": _w((lambda *s: torch.randn(*s, dtype=torch.bfloat16) * 0.05)(D, D)),
        }
    _A_in = _A_setup()

    def _B_setup():
        B, S, D, Dff = 1, 2048, 448, 1792
        return {
            "x": _w((lambda *s: torch.randn(*s, dtype=torch.bfloat16) * 0.05)(B, S, D)),
            "w1": _w((lambda *s: torch.randn(*s, dtype=torch.bfloat16) * 0.05)(D, Dff)),
            "w2": _w((lambda *s: torch.randn(*s, dtype=torch.bfloat16) * 0.05)(Dff, D)),
        }
    _B_in = _B_setup()

    return {
        "A_x": _A_in["x"],
        "A_wqkv": _A_in["wqkv"],
        "A_wo": _A_in["wo"],
        "B_x": _B_in["x"],
        "B_w1": _B_in["w1"],
        "B_w2": _B_in["w2"],
    }

def reference(A_x, A_wqkv, A_wo, B_x, B_w1, B_w2):
    def _A_ref(x, wqkv, wo):
        B, S, D = x.shape
        H = 16; Dh = D // H; W = 256
        qkv = (lambda x, *d: x.permute(*d))((x @ wqkv).reshape(B, S, 3, H, Dh), 2, 0, 3, 1, 4)
        q, k, v = qkv[0], qkv[1], qkv[2]
        scores = torch.einsum("bhqd,bhkd->bhqk", q, k) * (Dh ** -0.5)
        idx = torch.arange(S)
        mask = (idx[:, None] - idx[None, :] >= 0) & (idx[:, None] - idx[None, :] < W)
        scores = torch.where(mask[None, None], scores, -1e9)
        p = _softmax(scores, axis=-1)
        o = torch.einsum("bhqk,bhkd->bhqd", p, v)
        return (lambda x, *d: x.permute(*d))(o, 0, 2, 1, 3).reshape(B, S, D) @ wo

    def _B_ref(x, w1, w2):
        h = torch.einsum("bsd,df->bsf", x, w1)
        zero = torch.zeros_like(h)
        h = torch.maximum(h, zero)
        return torch.einsum("bsf,fd->bsd", h, w2)

    x = A_x
    h = x + _A_ref(_layernorm(x, axis=-1), A_wqkv, A_wo)
    h2 = h + _B_ref(_layernorm(h, axis=-1), B_w1, B_w2)
    return h2
```

```cpp
#include <hip/hip_runtime.h>
#include <hip/hip_bf16.h>
typedef unsigned short u16;
__device__ __forceinline__ float bf2f(u16 b){ return __uint_as_float(((unsigned)b)<<16); }
__device__ __forceinline__ u16 f2bf(float f){ unsigned u=__float_as_uint(f); u += 0x7FFFu + ((u>>16)&1u); return (u16)(u>>16); }
constexpr int S=2048, D=448, H=16, DH=28, W=256, F=1792;

__global__ void k_ln(const u16* x, u16* y){
  int row=blockIdx.x; int t=threadIdx.x;
  float v[7]; float s=0;
  for(int i=0;i<7;i++){ v[i]=bf2f(x[row*D+t+64*i]); s+=v[i]; }
  for(int o=32;o;o>>=1) s+=__shfl_xor(s,o);
  float m=s/D; float q=0;
  for(int i=0;i<7;i++){ float d=v[i]-m; q+=d*d; }
  for(int o=32;o;o>>=1) q+=__shfl_xor(q,o);
  float r=rsqrtf(q/D+1e-5f);
  for(int i=0;i<7;i++) y[row*D+t+64*i]=f2bf((v[i]-m)*r);
}
template<int RELU,int RES>
__global__ void k_gemm(const u16* A,const u16* B,const u16* R,u16* C,int M,int N,int K){
  int n=blockIdx.x*64+threadIdx.x; int m0=blockIdx.y*8;
  if(n>=N) return;
  float acc[8]={0,0,0,0,0,0,0,0};
  for(int k=0;k<K;k++){ float b=bf2f(B[(size_t)k*N+n]);
    #pragma unroll
    for(int i=0;i<8;i++) acc[i]+=bf2f(A[(size_t)(m0+i)*K+k])*b; }
  #pragma unroll
  for(int i=0;i<8;i++){ float v=acc[i]; if(RELU) v=fmaxf(v,0.f); if(RES) v=bf2f(f2bf(v))+bf2f(R[(size_t)(m0+i)*N+n]); C[(size_t)(m0+i)*N+n]=f2bf(v);}
}
__global__ void k_attn(const u16* qkv,u16* o){
  int q=blockIdx.x, h=blockIdx.y, t=threadIdx.x;
  __shared__ float p[256]; __shared__ float red[4];
  int key=q-255+t; float s=-1e30f;
  if(key>=0){ s=0; for(int d=0;d<DH;d++) s+=bf2f(qkv[q*3*D+h*DH+d])*bf2f(qkv[key*3*D+D+h*DH+d]); s*=rsqrtf((float)DH);}
  float m=s; for(int of=32;of;of>>=1) m=fmaxf(m,__shfl_xor(m,of));
  if((t&63)==0) red[t>>6]=m; __syncthreads(); m=fmaxf(fmaxf(red[0],red[1]),fmaxf(red[2],red[3])); __syncthreads();
  float e=key>=0? __expf(s-m):0.f; float l=e; for(int of=32;of;of>>=1) l+=__shfl_xor(l,of);
  if((t&63)==0) red[t>>6]=l; __syncthreads(); l=red[0]+red[1]+red[2]+red[3];
  p[t]=e/l; __syncthreads();
  if(t<DH){ float a=0; for(int j=0;j<256;j++){ int kk=q-255+j; if(kk>=0) a+=p[j]*bf2f(qkv[kk*3*D+2*D+h*DH+t]); } o[q*D+h*DH+t]=f2bf(a);}
}
extern "C" void kernel_launch(void* const* d_in,const int* in_sizes,int n_in,void* d_out,int out_size,void* d_ws,size_t ws_size,hipStream_t stream){
  const u16* x=(const u16*)d_in[0]; const u16* wqkv=(const u16*)d_in[1]; const u16* wo=(const u16*)d_in[2];
  const u16* w1=(const u16*)d_in[4]; const u16* w2=(const u16*)d_in[5];
  u16* ws=(u16*)d_ws; u16* xn=ws; u16* qkv=xn+S*D; u16* o=qkv+S*3*D; u16* h=o+S*D; u16* hn=h+S*D; u16* f=hn+S*D;
  k_ln<<<S,64,0,stream>>>(x,xn);
  k_gemm<0,0><<<dim3(21,S/8),64,0,stream>>>(xn,wqkv,nullptr,qkv,S,3*D,D);
  k_attn<<<dim3(S,H),256,0,stream>>>(qkv,o);
  k_gemm<0,1><<<dim3(7,S/8),64,0,stream>>>(o,wo,x,h,S,D,D);
  k_ln<<<S,64,0,stream>>>(h,hn);
  k_gemm<1,0><<<dim3(28,S/8),64,0,stream>>>(hn,w1,nullptr,f,S,F,D);
  k_gemm<0,1><<<dim3(7,S/8),64,0,stream>>>(f,w2,h,(u16*)d_out,S,D,F);
}
```

```cpp
#include <hip/hip_runtime.h>
#include <stdint.h>

namespace {
typedef unsigned v4u __attribute__((ext_vector_type(4)));
typedef unsigned v2u __attribute__((ext_vector_type(2)));
typedef float f32x4 __attribute__((ext_vector_type(4)));
typedef float f32x2 __attribute__((ext_vector_type(2)));
typedef __bf16 bf16x8 __attribute__((ext_vector_type(8)));
typedef __bf16 bf16x2 __attribute__((ext_vector_type(2)));
typedef short s16x4 __attribute__((ext_vector_type(4)));
#define LAS __attribute__((address_space(3)))
#define GAS __attribute__((address_space(1)))
typedef GAS unsigned long long gu64;
#define RLX_AGENT __ATOMIC_RELAXED, __HIP_MEMORY_SCOPE_AGENT
typedef __amdgpu_buffer_rsrc_t rsrc_t;

constexpr unsigned WS_EPOCH = 0;
constexpr unsigned WS_FLAG = 4096;
constexpr unsigned WS_STAMP = 512 * 1024;
constexpr unsigned SZ_ACT = 2048u * 448u * 2u;
constexpr unsigned WS_Q = 1u << 20;
constexpr unsigned WS_K = WS_Q + SZ_ACT;
constexpr unsigned WS_V = WS_K + SZ_ACT;
constexpr unsigned WS_O = WS_V + SZ_ACT;
constexpr unsigned WS_H = WS_O + (2u << 20);
constexpr unsigned WS_F = WS_H + SZ_ACT;
constexpr unsigned WS_END = WS_F + 32u * 56u * 4096u;
constexpr int SC1 = 16;
constexpr unsigned OOB = 0x7ffffff0u;

constexpr int L_XN = 0;
constexpr int L_WOF = 0;
constexpr int L_WR = 65536;
constexpr int L_KI = 65536;
constexpr int L_VI = 65536 + 24576;
constexpr int L_RED = 65536;
constexpr int L_HI = 65536;
constexpr int L_ST = 65536 + 57344;
constexpr int L_R = 0;
constexpr int L_XS = 131072;
constexpr int L_HS = 131072 + 9216;
constexpr int L_TOTAL = 131072 + 2 * 9216;

__device__ __forceinline__ f32x4 mfma(v4u a, v4u b, f32x4 c) {
  return __builtin_amdgcn_mfma_f32_16x16x32_bf16(__builtin_bit_cast(bf16x8, a), __builtin_bit_cast(bf16x8, b), c, 0, 0, 0);
}
__device__ __forceinline__ unsigned pk(float lo, float hi) {
  f32x2 f = {lo, hi};
  return __builtin_bit_cast(unsigned, __builtin_convertvector(f, bf16x2));
}
__device__ __forceinline__ float blo(unsigned v) { return __uint_as_float(v << 16); }
__device__ __forceinline__ float bhi(unsigned v) { return __uint_as_float(v & 0xffff0000u); }
__device__ __forceinline__ float dot2(unsigned a, unsigned b, float c) {
  return __builtin_amdgcn_fdot2_f32_bf16(__builtin_bit_cast(bf16x2, a), __builtin_bit_cast(bf16x2, b), c, false);
}
template <int CTRL, int BANK> __device__ __forceinline__ unsigned dpp(unsigned old, unsigned src) {
  return __builtin_amdgcn_update_dpp(old, src, CTRL, 0xf, BANK, false);
}
template <int CTRL> __device__ __forceinline__ float dppf(float v) {
  return __uint_as_float(__builtin_amdgcn_update_dpp(0u, __float_as_uint(v), CTRL, 0xf, 0xf, true));
}
__device__ __forceinline__ v4u ld(rsrc_t r, unsigned off) { return __builtin_amdgcn_raw_buffer_load_b128(r, off, 0, 0); }
__device__ __forceinline__ v4u ldc(rsrc_t r, unsigned off) { return __builtin_amdgcn_raw_buffer_load_b128(r, off, 0, SC1); }
__device__ __forceinline__ void stc(rsrc_t r, unsigned off, v4u v) { __builtin_amdgcn_raw_buffer_store_b128(v, r, off, 0, SC1); }
__device__ __forceinline__ rsrc_t mkrs(const void* p, unsigned n) { return __builtin_amdgcn_make_buffer_rsrc((void*)p, 0, n, 0x00020000); }
#define SBAR() __builtin_amdgcn_sched_barrier(0)
#define LBAR() do { asm volatile("s_waitcnt lgkmcnt(0)" ::: "memory"); __builtin_amdgcn_s_barrier(); asm volatile("" ::: "memory"); } while (0)
#define DRAIN() asm volatile("s_waitcnt vmcnt(0)" ::: "memory")
constexpr unsigned PLO = 0x05040100u, PHI = 0x07060302u;
__device__ __forceinline__ unsigned prm(unsigned hi, unsigned lo, unsigned sel) { return __builtin_amdgcn_perm(hi, lo, sel); }

__device__ __forceinline__ void xform64(const v4u (&X)[4], v4u (&F)[4]) {
  unsigned nx[4], ny[4], nz[4], nw[4];
#pragma unroll
  for (int i = 0; i < 4; ++i) {
    nz[i] = dpp<0x128, 0x3>(X[i].z, X[i].x);
    nw[i] = dpp<0x128, 0x3>(X[i].w, X[i].y);
    nx[i] = dpp<0x128, 0xC>(X[i].x, X[i].z);
    ny[i] = dpp<0x128, 0xC>(X[i].y, X[i].w);
  }
  F[0] = v4u{prm(nx[1], nx[0], PLO), prm(nx[3], nx[2], PLO), prm(nz[1], nz[0], PLO), prm(nz[3], nz[2], PLO)};
  F[1] = v4u{prm(nx[1], nx[0], PHI), prm(nx[3], nx[2], PHI), prm(nz[1], nz[0], PHI), prm(nz[3], nz[2], PHI)};
  F[2] = v4u{prm(ny[1], ny[0], PLO), prm(ny[3], ny[2], PLO), prm(nw[1], nw[0], PLO), prm(nw[3], nw[2], PLO)};
  F[3] = v4u{prm(ny[1], ny[0], PHI), prm(ny[3], ny[2], PHI), prm(nw[1], nw[0], PHI), prm(nw[3], nw[2], PHI)};
}
__device__ __forceinline__ void xform32(v4u (&X)[2]) {
  unsigned ax[2], ay[2], az[2], aw[2];
#pragma unroll
  for (int i = 0; i < 2; ++i) {
    const unsigned nz = dpp<0x128, 0x3>(X[i].z, X[i].x), nw = dpp<0x128, 0x3>(X[i].w, X[i].y);
    const unsigned nx = dpp<0x128, 0xC>(X[i].x, X[i].z), ny = dpp<0x128, 0xC>(X[i].y, X[i].w);
    ay[i] = dpp<0x12C, 0x5>(ny, nx);
    aw[i] = dpp<0x12C, 0x5>(nw, nz);
    ax[i] = dpp<0x124, 0xA>(nx, ny);
    az[i] = dpp<0x124, 0xA>(nz, nw);
  }
  X[0] = v4u{prm(ax[1], ax[0], PLO), prm(ay[1], ay[0], PLO), prm(az[1], az[0], PLO), prm(aw[1], aw[0], PLO)};
  X[1] = v4u{prm(ax[1], ax[0], PHI), prm(ay[1], ay[0], PHI), prm(az[1], az[0], PHI), prm(aw[1], aw[0], PHI)};
}
__device__ __forceinline__ float xmax16_32(float v) {
  auto a = __builtin_amdgcn_permlane16_swap(__float_as_uint(v), __float_as_uint(v), false, false);
  v = fmaxf(__uint_as_float(a[0]), __uint_as_float(a[1]));
  auto b = __builtin_amdgcn_permlane32_swap(__float_as_uint(v), __float_as_uint(v), false, false);
  return fmaxf(__uint_as_float(b[0]), __uint_as_float(b[1]));
}
__device__ __forceinline__ float xsum16_32(float v) {
  auto a = __builtin_amdgcn_permlane16_swap(__float_as_uint(v), __float_as_uint(v), false, false);
  v = __uint_as_float(a[0]) + __uint_as_float(a[1]);
  auto b = __builtin_amdgcn_permlane32_swap(__float_as_uint(v), __float_as_uint(v), false, false);
  return __uint_as_float(b[0]) + __uint_as_float(b[1]);
}

__device__ __forceinline__ void wait_flags(unsigned char* ws, int edge, int idx, unsigned long long tag, int w) {
  if (w == 0) {
    gu64* p = (gu64*)(ws + WS_FLAG + edge * 32768 + (idx < 0 ? 0 : idx) * 128);
    for (unsigned spin = 0; spin < (1u << 17); ++spin) {
      const unsigned long long v = __hip_atomic_load(p, RLX_AGENT);
      if (__all(idx < 0 || v == tag)) break;
      __builtin_amdgcn_s_sleep(1);
    }
  }
  LBAR();
}
__device__ __forceinline__ void publish(unsigned char* ws, int edge, int idx, unsigned long long tag, int tid) {
  if (tid == 0) __hip_atomic_store((gu64*)(ws + WS_FLAG + edge * 32768 + idx * 128), tag, RLX_AGENT);
}

#define STAMP(k) do {} while (0)

__global__ void __launch_bounds__(512, 2)
fused_block(const unsigned char* __restrict__ gx, const unsigned char* __restrict__ gwqkv, const unsigned char* __restrict__ gwo,
            const unsigned char* __restrict__ gw1, const unsigned char* __restrict__ gw2, unsigned char* __restrict__ gout,
            unsigned char* ws) {
  __shared__ __attribute__((aligned(16))) unsigned char smem[L_TOTAL];
  LAS unsigned char* L = (LAS unsigned char*)smem;
  const int tid = threadIdx.x, lane = tid & 63;
  const int w = __builtin_amdgcn_readfirstlane(tid >> 6);
  const int bid = blockIdx.x, xcd = bid & 7, slot = bid >> 3;
  const bool gemm = slot < 28;
  const int rb = 4 * xcd + slot / 7, j = slot % 7;
  const int hd = 2 * xcd + (slot >> 4), ab = slot & 15;
  const int tok = lane & 15, g = lane >> 4;
  STAMP(0);
  unsigned cpf = 0;
  {
    unsigned long long pc;
    unsigned len;
    asm volatile("s_getpc_b64 %0\n.Lfb_here:\n\ts_mov_b32 %1, .Lfb_end-.Lfb_here" : "=s"(pc), "=s"(len));
    const unsigned o = (unsigned)(slot * 16 + tid) * 128u;
    if (tid < 16 && o < len) cpf = *(const GAS unsigned*)((pc & ~3ull) + o);
  }
  const rsrc_t rX = mkrs(gx, SZ_ACT), rWQ = mkrs(gwqkv, 448u * 2688u), rWO = mkrs(gwo, 448u * 896u);
  const rsrc_t rW1 = mkrs(gw1, 448u * 3584u), rW2 = mkrs(gw2, 1792u * 896u), rWS = mkrs(ws, WS_END), rOUT = mkrs(gout, SZ_ACT);
  unsigned long long tag = 0;

  if (gemm) {
    const int row8 = tid >> 3, c8 = tid & 7;
    v4u xr[7], wr[7][3];
    const unsigned xo = (unsigned)(rb * 64 + row8) * 896u + c8 * 16u;
#pragma unroll
    for (int i = 0; i < 7; ++i) xr[i] = ld(rX, xo + i * 128u);
    const v4u xs = ld(rX, xo + j * 128u);
    SBAR();
    __builtin_amdgcn_s_barrier();
    const unsigned wo_ = (unsigned)row8 * 2688u + j * 128u + c8 * 16u;
#pragma unroll
    for (int p = 0; p < 3; ++p)
#pragma unroll
      for (int s = 0; s < 3; ++s) wr[p][s] = ld(rWQ, wo_ + p * (64u * 2688u) + s * 896u);
    if (w == 0) tag = __hip_atomic_load((gu64*)(ws + WS_EPOCH), RLX_AGENT) + 1ull;
    SBAR();
    float s1 = 0.f, s2 = 0.f;
#pragma unroll
    for (int i = 0; i < 7; ++i)
#pragma unroll
      for (int d = 0; d < 4; ++d) { s1 = dot2(xr[i][d], 0x3f803f80u, s1); s2 = dot2(xr[i][d], xr[i][d], s2); }
    s1 += dppf<0xb1>(s1); s2 += dppf<0xb1>(s2);
    s1 += dppf<0x4e>(s1); s2 += dppf<0x4e>(s2);
    s1 += dppf<0x141>(s1); s2 += dppf<0x141>(s2);
    const float mean = s1 * (1.0f / 448.0f);
    const float rstd = __builtin_amdgcn_rsqf(fmaxf(s2 * (1.0f / 448.0f) - mean * mean, 0.f) + 1e-5f);
    const float nb = -mean * rstd;
    {
      const int mt = row8 >> 4, tk = row8 & 15;
      const int base = L_XN + (((c8 >> 2) * 4 + mt) * 64 + (c8 & 3) * 16 + (tk ^ c8)) * 16;
#pragma unroll
      for (int i = 0; i < 7; ++i) {
        v4u o;
#pragma unroll
        for (int d = 0; d < 4; ++d) o[d] = pk(fmaf(blo(xr[i][d]), rstd, nb), fmaf(bhi(xr[i][d]), rstd, nb));
        *(LAS v4u*)(L + base + i * 8192) = o;
      }
      *(LAS v4u*)(L + L_XS + row8 * 144 + c8 * 16) = xs;
    }
    asm volatile("" ::"v"(cpf));
    STAMP(1);
    const int fw = ((row8 >> 1) & 1) | (((row8 >> 3) & 1) << 1);
    const int wbase = L_WR + row8 * 128 + ((c8 ^ (fw << 1)) * 16);
    const int mp = w & 1, qt = w >> 1;
    const int q4 = (lane >> 2) & 3, p4 = lane & 3;
    const int fr = ((q4 >> 1) & 1) | ((g & 1) << 1);
    const int trow = L_WR + (8 * g + q4) * 128 + (p4 >> 1) * 16 + (p4 & 1) * 8;
    const int cs = qt & 1;
    const int ta0 = trow + (qt >> 1) * 8192 + (((2 * cs) ^ fr) * 32);
    const int ta1 = trow + (qt >> 1) * 8192 + (((2 * cs + 1) ^ fr) * 32);
    const int tav = trow + 2 * 8192 + ((qt ^ fr) * 32);
    const int xa0 = L_XN + (2 * mp * 64 + g * 16 + (tok ^ g)) * 16;
    const int xa1 = L_XN + 4096 + (2 * mp * 64 + g * 16 + (tok ^ (g + 4))) * 16;
    f32x4 aq[2][2], av[2];
#pragma unroll
    for (int m = 0; m < 2; ++m) { aq[m][0] = f32x4{0, 0, 0, 0}; aq[m][1] = f32x4{0, 0, 0, 0}; av[m] = f32x4{0, 0, 0, 0}; }
#pragma unroll
    for (int s = 0; s < 3; ++s) *(LAS v4u*)(L + wbase + s * 8192) = wr[0][s];
    LBAR();
#pragma unroll
    for (int p = 0; p < 7; ++p) {
      if (p + 3 < 7) {
#pragma unroll
        for (int s = 0; s < 3; ++s) wr[p + 3][s] = ld(rWQ, wo_ + (p + 3) * (64u * 2688u) + s * 896u);
        SBAR();
      }
      const int sl = (p & 1) * 24576;
#pragma unroll
      for (int kk = 0; kk < 2; ++kk) {
        auto trf = [&](int a) {
          const v2u lo = __builtin_bit_cast(v2u, __builtin_amdgcn_ds_read_tr16_b64_v4i16((LAS s16x4*)(L + a + sl + kk * 4096)));
          const v2u hi = __builtin_bit_cast(v2u, __builtin_amdgcn_ds_read_tr16_b64_v4i16((LAS s16x4*)(L + a + sl + kk * 4096 + 512)));
          return v4u{lo.x, lo.y, hi.x, hi.y};
        };
        const v4u f0 = trf(ta0), f1 = trf(ta1), fv = trf(tav);
        const int xa = (kk ? xa1 : xa0) + p * 8192;
        const v4u x0 = *(LAS v4u*)(L + xa), x1 = *(LAS v4u*)(L + xa + 1024);
        aq[0][0] = mfma(f0, x0, aq[0][0]); aq[0][1] = mfma(f1, x0, aq[0][1]); av[0] = mfma(x0, fv, av[0]);
        aq[1][0] = mfma(f0, x1, aq[1][0]); aq[1][1] = mfma(f1, x1, aq[1][1]); av[1] = mfma(x1, fv, av[1]);
      }
      if (p + 1 < 7) {
#pragma unroll
        for (int s = 0; s < 3; ++s) *(LAS v4u*)(L + wbase + ((p + 1) & 1) * 24576 + s * 8192) = wr[p + 1][s];
        LBAR();
      }
    }
    STAMP(2);
    const float sc = (qt < 2) ? 0.18898223650461362f * 1.4426950408889634f : 1.0f;
    const unsigned qkb = ((qt < 2) ? WS_Q : WS_K) + (unsigned)(8 * j + 4 * cs + (((g & 1) << 1) | (g >> 1))) * 32768u + (unsigned)(rb * 64 + mp * 32 + tok) * 16u;
#pragma unroll
    for (int m = 0; m < 2; ++m) {
      const f32x4 a = aq[m][0] * sc, b = aq[m][1] * sc;
      auto s0 = __builtin_amdgcn_permlane16_swap(pk(a[0], a[1]), pk(b[0], b[1]), false, false);
      auto s1_ = __builtin_amdgcn_permlane16_swap(pk(a[2], a[3]), pk(b[2], b[3]), false, false);
      stc(rWS, qkb + m * 256u, v4u{s0[0], s1_[0], s0[1], s1_[1]});
    }
    stc(rWS, WS_V + (unsigned)((2 * rb + mp) * 4 + g) * 7168u + (unsigned)(64 * j + 16 * qt + tok) * 16u,
        v4u{pk(av[0][0], av[0][1]), pk(av[0][2], av[0][3]), pk(av[1][0], av[1][1]), pk(av[1][2], av[1][3])});
    DRAIN();
    LBAR();
    publish(ws, 0, rb * 7 + j, tag, tid);
    STAMP(3);
  } else {
    if (w == 0) tag = __hip_atomic_load((gu64*)(ws + WS_EPOCH), RLX_AGENT) + 1ull;
    asm volatile("" ::"v"(cpf));
  }

  v4u w1f[14][2];
  const unsigned w1o = (unsigned)(32 * (g & 1) + 8 * ((lane >> 2) & 3) + 4 * (g >> 1)) * 3584u + (unsigned)(256 * j + 32 * w + 8 * (lane & 3)) * 2u;
  if (gemm) {
    v4u wor[2][4];
    const int b3 = (lane >> 3) & 1;
#pragma unroll
    for (int hh = 0; hh < 2; ++hh)
#pragma unroll
      for (int i = 0; i < 4; ++i) {
        const int d = 16 * b3 + 4 * g + i;
        wor[hh][i] = ld(rWO, (unsigned)(28 * (2 * w + hh) + (d < 28 ? d : 27)) * 896u + j * 128u + (lane & 7) * 16u);
      }
#pragma unroll
    for (int k = 0; k < 4; ++k)
#pragma unroll
      for (int i = 0; i < 2; ++i) w1f[k][i] = ld(rW1, w1o + (unsigned)(64 * (k >> 1) + 2 * (k & 1) + i) * 3584u);
    SBAR();
#pragma unroll
    for (int hh = 0; hh < 2; ++hh) {
      v4u F[4];
      xform64(wor[hh], F);
#pragma unroll
      for (int n = 0; n < 4; ++n) *(LAS v4u*)(L + L_WOF + ((2 * w + hh) * 4 + n) * 1024 + lane * 16) = F[n];
    }
  }

  {
    f32x4 s[18];
    const int lg0 = w >> 1;
    {
      const int qpos = 256 + 16 * w + tok;
      const int kmin = (ab < 2) ? (256 - 128 * ab) : 0;
#pragma unroll
      for (int t = 0; t < 18; ++t)
#pragma unroll
        for (int r = 0; r < 4; ++r) {
          const int kpos = 32 * lg0 + 16 * t + 4 * g + r;
          const int df = qpos - kpos;
          s[t][r] = (df >= 0 && df < 256 && kpos >= kmin) ? 0.f : -__builtin_inff();
        }
    }
    {
      const int j0 = (28 * hd) >> 6, j1 = (28 * hd + 27) >> 6;
      const int r_ = 2 * ab - 4 + (lane >> 1), jj = (lane & 1) ? j1 : j0;
      const int idx = (lane < 12 && r_ >= 0) ? r_ * 7 + jj : -1;
      wait_flags(ws, 0, idx, tag, w);
    }
    STAMP(4);
    const int c0 = (7 * hd - (hd & 1)) >> 1;
    v4u qf = ldc(rWS, WS_Q + (unsigned)(c0 + g) * 32768u + (unsigned)(128 * ab + 16 * w + tok) * 16u);
    v4u kr[3], vr[3];
#pragma unroll
    for (int i = 0; i < 3; ++i) {
      const int tk = 128 * ab - 256 + 16 * (w + 8 * i) + tok;
      kr[i] = ldc(rWS, tk >= 0 ? WS_K + (unsigned)(c0 + g) * 32768u + (unsigned)tk * 16u : OOB);
    }
#pragma unroll
    for (int i = 0; i < 3; ++i) {
      const int u = w + 8 * i, lg = u >> 1, dt = u & 1;
      const int tg = 4 * ab - 8 + lg, d = 16 * dt + tok;
      vr[i] = ldc(rWS, (tg >= 0 && d < 28) ? WS_V + (unsigned)(tg * 4 + g) * 7168u + (unsigned)(28 * hd + d) * 16u : OOB);
    }
    SBAR();
    if (gemm) {
#pragma unroll
      for (int k = 4; k < 14; ++k)
#pragma unroll
        for (int i = 0; i < 2; ++i) w1f[k][i] = ld(rW1, w1o + (unsigned)(64 * (k >> 1) + 2 * (k & 1) + i) * 3584u);
    }
    SBAR();
#pragma unroll
    for (int i = 0; i < 3; ++i) *(LAS v4u*)(L + L_KI + (w + 8 * i) * 1024 + lane * 16) = kr[i];
    const bool onesl = (w & 1) && tok >= 12;
#pragma unroll
    for (int i = 0; i < 3; ++i) {
      v4u v = vr[i];
      if (onesl) v = v4u{0x3f803f80u, 0x3f803f80u, 0x3f803f80u, 0x3f803f80u};
      *(LAS v4u*)(L + L_VI + (w + 8 * i) * 1024 + lane * 16) = v;
    }
    if (hd & 1) { if (g == 0) { qf.x = 0; qf.y = 0; } } else { if (g == 3) { qf.z = 0; qf.w = 0; } }
    LBAR();
    STAMP(5);
    const int kb = L_KI + lg0 * 2048 + lane * 16, vb = L_VI + lg0 * 2048 + lane * 16;
#pragma unroll
    for (int t = 0; t < 18; ++t) s[t] = mfma(*(LAS v4u*)(L + kb + t * 1024), qf, s[t]);
    float mx = -__builtin_inff();
#pragma unroll
    for (int t = 0; t < 18; ++t) mx = fmaxf(fmaxf(mx, fmaxf(s[t][0], s[t][1])), fmaxf(s[t][2], s[t][3]));
    mx = xmax16_32(mx);
    f32x4 o0 = {0, 0, 0, 0}, o1 = {0, 0, 0, 0};
#pragma unroll
    for (int gi = 0; gi < 9; ++gi) {
      float e[8];
#pragma unroll
      for (int r = 0; r < 4; ++r) { e[r] = __builtin_amdgcn_exp2f(s[2 * gi][r] - mx); e[4 + r] = __builtin_amdgcn_exp2f(s[2 * gi + 1][r] - mx); }
      const v4u pf = {pk(e[0], e[1]), pk(e[2], e[3]), pk(e[4], e[5]), pk(e[6], e[7])};
      o0 = mfma(*(LAS v4u*)(L + vb + gi * 2048), pf, o0);
      o1 = mfma(*(LAS v4u*)(L + vb + gi * 2048 + 1024), pf, o1);
    }
    const float den = __shfl(o1[0], tok + 48);
    const float inv = __builtin_amdgcn_rcpf(den);
    o0 *= inv; o1 *= inv;
    v4u ov = {pk(o0[0], o0[1]), pk(o0[2], o0[3]), pk(o1[0], o1[1]), pk(o1[2], o1[3])};
    if (g == 3) { ov.z = 0; ov.w = 0; }
    STAMP(6);
    stc(rWS, WS_O + (unsigned)(((2 * ab + (w >> 2)) * 16 + hd) * 4 + (w & 3)) * 1024u + lane * 16u, ov);
    DRAIN();
    LBAR();
    publish(ws, 1, ab * 16 + hd, tag, tid);
    STAMP(7);
  }
  if (!gemm) {
    if (bid == 255 && w == 0) {
#pragma unroll 1
      for (int q = 0; q < 4; ++q) {
        gu64* p = (gu64*)(ws + WS_FLAG + 32768 + (q * 64 + lane) * 128);
        for (unsigned spin = 0; spin < (1u << 17); ++spin) {
          if (__all(__hip_atomic_load(p, RLX_AGENT) == tag)) break;
          __builtin_amdgcn_s_sleep(8);
        }
      }
      if (lane == 0) __hip_atomic_store((gu64*)(ws + WS_EPOCH), tag, RLX_AGENT);
    }
    return;
  }

  v4u w2r[7][4];
  const unsigned w2o = (unsigned)(32 * w + 2 * g + ((lane >> 3) & 1)) * 896u + j * 128u + (lane & 7) * 16u;
#pragma unroll
  for (int s = 0; s < 3; ++s)
#pragma unroll
    for (int i = 0; i < 4; ++i) w2r[s][i] = ld(rW2, w2o + (unsigned)(256 * s + 8 * i) * 896u);
  SBAR();
  f32x4 cs0 = {0, 0, 0, 0}, cs1 = {0, 0, 0, 0};
  {
    const v4u ones = {0x3f803f80u, 0x3f803f80u, 0x3f803f80u, 0x3f803f80u};
#pragma unroll
    for (int k = 0; k < 14; ++k) {
      xform32(w1f[k]);
      cs0 = mfma(w1f[k][0], ones, cs0);
      cs1 = mfma(w1f[k][1], ones, cs1);
    }
  }

  {
    const int tt = w & 3, kh = w >> 2;
    wait_flags(ws, 1, lane < 16 ? (rb >> 1) * 16 + lane : -1, tag, w);
    STAMP(8);
    v4u of[8];
#pragma unroll
    for (int i = 0; i < 8; ++i) of[i] = ldc(rWS, WS_O + (unsigned)((rb * 16 + 8 * kh + i) * 4 + tt) * 1024u + lane * 16u);
    SBAR();
    f32x4 a[4];
#pragma unroll
    for (int n = 0; n < 4; ++n) a[n] = f32x4{0, 0, 0, 0};
    const int wb = L_WOF + kh * 32768 + lane * 16;
#pragma unroll
    for (int i = 0; i < 8; ++i)
#pragma unroll
      for (int n = 0; n < 4; ++n) a[n] = mfma(*(LAS v4u*)(L + wb + (i * 4 + n) * 1024), of[i], a[n]);
    STAMP(9);
    const f32x4 s0 = kh ? a[0] : a[2], s1 = kh ? a[1] : a[3];
    f32x4 k0 = kh ? a[2] : a[0], k1 = kh ? a[3] : a[1];
    *(LAS f32x4*)(L + L_RED + w * 2048 + lane * 16) = s0;
    *(LAS f32x4*)(L + L_RED + w * 2048 + 1024 + lane * 16) = s1;
    LBAR();
    k0 += *(LAS f32x4*)(L + L_RED + (w ^ 4) * 2048 + lane * 16);
    k1 += *(LAS f32x4*)(L + L_RED + (w ^ 4) * 2048 + 1024 + lane * 16);
    const int so = (16 * tt + tok) * 144 + (32 * (g & 1) + 4 * (g >> 1) + 2 * kh) * 2;
    v4u hv;
#pragma unroll
    for (int r = 0; r < 4; ++r) {
      const unsigned xv = *(LAS unsigned*)(L + L_XS + so + r * 16);
      hv[r] = pk(blo(xv) + k0[r], bhi(xv) + k1[r]);
      *(LAS unsigned*)(L + L_HS + so + r * 16) = hv[r];
    }
    stc(rWS, WS_H + (unsigned)((rb * 14 + 2 * j + kh) * 4 + tt) * 1024u + lane * 16u, hv);
    DRAIN();
    LBAR();
    publish(ws, 2, rb * 7 + j, tag, tid);
    STAMP(10);
  }

  v4u ff[4];
  {
    wait_flags(ws, 2, lane < 7 ? rb * 7 + lane : -1, tag, w);
    STAMP(11);
    const int m_ = w & 3, kh = w >> 2;
    v4u hr[7];
#pragma unroll
    for (int i = 0; i < 7; ++i) hr[i] = ldc(rWS, WS_H + (unsigned)((rb * 14 + 7 * kh + i) * 4 + m_) * 1024u + lane * 16u);
    SBAR();
    float s1 = 0.f, s2 = 0.f;
#pragma unroll
    for (int i = 0; i < 7; ++i) {
      *(LAS v4u*)(L + L_HI + ((7 * kh + i) * 4 + m_) * 1024 + lane * 16) = hr[i];
#pragma unroll
      for (int d = 0; d < 4; ++d) { s1 = dot2(hr[i][d], 0x3f803f80u, s1); s2 = dot2(hr[i][d], hr[i][d], s2); }
    }
    s1 = xsum16_32(s1); s2 = xsum16_32(s2);
    if (g == 0) *(LAS f32x2*)(L + L_ST + (kh * 64 + 16 * m_ + tok) * 8) = f32x2{s1, s2};
    LBAR();
    STAMP(12);
    f32x4 a[4][2];
#pragma unroll
    for (int m = 0; m < 4; ++m) { a[m][0] = f32x4{0, 0, 0, 0}; a[m][1] = f32x4{0, 0, 0, 0}; }
#pragma unroll
    for (int k = 0; k < 14; ++k) {
#pragma unroll
      for (int m = 0; m < 4; ++m) {
        const v4u hf = *(LAS v4u*)(L + L_HI + (k * 4 + m) * 1024 + lane * 16);
        a[m][0] = mfma(w1f[k][0], hf, a[m][0]);
        a[m][1] = mfma(w1f[k][1], hf, a[m][1]);
      }
      if (k >= 2 && k < 10 && (k & 1) == 0) {
        const int s = 3 + (k - 2) / 2;
#pragma unroll
        for (int i = 0; i < 4; ++i) w2r[s][i] = ld(rW2, w2o + (unsigned)(256 * s + 8 * i) * 896u);
        SBAR();
      }
    }
    STAMP(13);
#pragma unroll
    for (int m = 0; m < 4; ++m) {
      const f32x2 p0 = *(LAS f32x2*)(L + L_ST + (16 * m + tok) * 8), p1 = *(LAS f32x2*)(L + L_ST + (64 + 16 * m + tok) * 8);
      const float mean = (p0.x + p1.x) * (1.0f / 448.0f);
      const float rstd = __builtin_amdgcn_rsqf(fmaxf((p0.y + p1.y) * (1.0f / 448.0f) - mean * mean, 0.f) + 1e-5f);
      float f0[4], f1[4];
#pragma unroll
      for (int r = 0; r < 4; ++r) {
        f0[r] = fmaxf(fmaf(-mean, cs0[r], a[m][0][r]) * rstd, 0.f);
        f1[r] = fmaxf(fmaf(-mean, cs1[r], a[m][1][r]) * rstd, 0.f);
      }
      ff[m] = v4u{pk(f0[0], f0[1]), pk(f0[2], f0[3]), pk(f1[0], f1[1]), pk(f1[2], f1[3])};
      stc(rWS, WS_F + (unsigned)((rb * 56 + 8 * j + w) * 4 + m) * 1024u + lane * 16u, ff[m]);
    }
    DRAIN();
    LBAR();
    publish(ws, 3, rb * 7 + j, tag, tid);
    STAMP(14);
  }

  {
#pragma unroll
    for (int s = 0; s < 7; ++s) { v4u F[4]; xform64(w2r[s], F);
#pragma unroll
      for (int n = 0; n < 4; ++n) w2r[s][n] = F[n]; }
    wait_flags(ws, 3, lane < 7 ? rb * 7 + lane : -1, tag, w);
    STAMP(15);
    f32x4 a[4][4];
#pragma unroll
    for (int m = 0; m < 4; ++m)
#pragma unroll
      for (int n = 0; n < 4; ++n) a[m][n] = f32x4{0, 0, 0, 0};
    const unsigned fo = WS_F + (unsigned)(rb * 56 + w) * 4096u + lane * 16u;
    v4u fr[7][4];
#pragma unroll
    for (int s = 0; s < 3; ++s)
#pragma unroll
      for (int m = 0; m < 4; ++m) fr[s][m] = ldc(rWS, fo + s * 32768u + m * 1024u);
    SBAR();
#pragma unroll
    for (int s = 0; s < 7; ++s) {
#pragma unroll
      for (int m = 0; m < 4; ++m)
#pragma unroll
        for (int n = 0; n < 4; ++n) a[m][n] = mfma(fr[s][m], w2r[s][n], a[m][n]);
      if (s + 3 < 7) {
#pragma unroll
        for (int m = 0; m < 4; ++m) fr[s + 3][m] = ldc(rWS, fo + (s + 3) * 32768u + m * 1024u);
      }
      SBAR();
    }
    STAMP(16);
#pragma unroll
    for (int m = 0; m < 4; ++m)
#pragma unroll
      for (int r = 0; r < 4; ++r) {
        const int tk = 16 * m + 4 * g + r;
        *(LAS f32x4*)(L + L_R + w * 16384 + tk * 256 + ((tok ^ ((r & 2) << 2)) * 16)) = f32x4{a[m][0][r], a[m][1][r], a[m][2][r], a[m][3][r]};
      }
    LBAR();
    STAMP(17);
    const int tk = tid >> 3, ca = tid & 7;
    const int ra = L_R + tk * 256 + ((ca ^ ((tk & 2) << 2)) * 16);
    f32x4 y0 = {0, 0, 0, 0}, y1 = {0, 0, 0, 0};
#pragma unroll
    for (int p = 0; p < 8; ++p) { y0 += *(LAS f32x4*)(L + ra + p * 16384); y1 += *(LAS f32x4*)(L + (ra ^ 128) + p * 16384); }
    const v4u hv = *(LAS v4u*)(L + L_HS + tk * 144 + ca * 16);
    const v4u ov = {pk(blo(hv.x) + y0[0], bhi(hv.x) + y0[1]), pk(blo(hv.y) + y0[2], bhi(hv.y) + y0[3]),
                    pk(blo(hv.z) + y1[0], bhi(hv.z) + y1[1]), pk(blo(hv.w) + y1[2], bhi(hv.w) + y1[3])};
    __builtin_amdgcn_raw_buffer_store_b128(ov, rOUT, (unsigned)(rb * 64 + tk) * 896u + j * 128u + ca * 16u, 0, 0);
    STAMP(18);
  }
  asm volatile(".Lfb_end:" ::: "memory");
}
}

extern "C" void kernel_launch(void* const* d_in, const int* in_sizes, int n_in, void* d_out, int out_size, void* d_ws, size_t ws_size,
                              hipStream_t stream) {
  fused_block<<<256, 512, 0, stream>>>((const unsigned char*)d_in[0], (const unsigned char*)d_in[1], (const unsigned char*)d_in[2],
                                       (const unsigned char*)d_in[4], (const unsigned char*)d_in[5], (unsigned char*)d_out,
                                       (unsigned char*)d_ws);
}
```

```cpp
#include <hip/hip_runtime.h>
#include <stdint.h>

#define WARM 1
#define WARM_DELAY 0
#define FULLLINE 1
#define PRETOUCH 0
namespace {
typedef unsigned v4u __attribute__((ext_vector_type(4)));
typedef unsigned v2u __attribute__((ext_vector_type(2)));
typedef float f32x4 __attribute__((ext_vector_type(4)));
typedef float f32x2 __attribute__((ext_vector_type(2)));
typedef __bf16 bf16x8 __attribute__((ext_vector_type(8)));
typedef __bf16 bf16x2 __attribute__((ext_vector_type(2)));
typedef short s16x4 __attribute__((ext_vector_type(4)));
#define LAS __attribute__((address_space(3)))
#define GAS __attribute__((address_space(1)))
typedef GAS unsigned long long gu64;
#define RLX_AGENT __ATOMIC_RELAXED, __HIP_MEMORY_SCOPE_AGENT
typedef __amdgpu_buffer_rsrc_t rsrc_t;

constexpr unsigned WS_EPOCH = 0;
constexpr unsigned WS_F0 = 4096;
constexpr unsigned WS_F1 = WS_F0 + 256 * 128;
constexpr unsigned WS_F2 = WS_F1 + 2048 * 128;
constexpr unsigned WS_F3 = WS_F2 + 2048 * 128;
constexpr unsigned WS_STAMP = WS_F3 + 4096 * 128;
constexpr unsigned SZ_ACT = 2048u * 448u * 2u;
constexpr unsigned WS_Q = 2u << 20;
constexpr unsigned WS_K = WS_Q + SZ_ACT;
constexpr unsigned WS_V = WS_K + SZ_ACT;
constexpr unsigned WS_O = WS_V + SZ_ACT;
constexpr unsigned WS_H = WS_O + (2u << 20);
constexpr unsigned WS_HS = WS_H + SZ_ACT;
constexpr unsigned WS_F = WS_HS + 32u * 8192u;
constexpr unsigned WS_END = WS_F + 32u * 56u * 4096u;
constexpr int SC1 = 16;
constexpr unsigned OOB = 0x40000000u;

constexpr int L_RING = 0;
constexpr int L_WO = 0;
constexpr int L_KI = 65536;
constexpr int L_VI = 65536 + 24576;
constexpr int L_QI = 65536 + 49152;
constexpr int L_HI = 65536;
constexpr int L_RED0 = 65536 + 57344;
constexpr int L_R = 0;
constexpr int L_XS = 131072;
constexpr int L_HS = L_XS + 9216;
constexpr int L_CS = L_HS + 9216;
constexpr int L_ST = L_CS + 1536;
constexpr int L_CNT = L_ST + 1024;
constexpr int L_RED1 = L_CNT + 16;
constexpr int L_TOTAL = L_RED1 + 8192;

__device__ __forceinline__ f32x4 mfma(v4u a, v4u b, f32x4 c) {
  return __builtin_amdgcn_mfma_f32_16x16x32_bf16(__builtin_bit_cast(bf16x8, a), __builtin_bit_cast(bf16x8, b), c, 0, 0, 0);
}
__device__ __forceinline__ unsigned pk(float lo, float hi) {
  f32x2 f = {lo, hi};
  return __builtin_bit_cast(unsigned, __builtin_convertvector(f, bf16x2));
}
__device__ __forceinline__ float blo(unsigned v) { return __uint_as_float(v << 16); }
__device__ __forceinline__ float bhi(unsigned v) { return __uint_as_float(v & 0xffff0000u); }
__device__ __forceinline__ float dot2(unsigned a, unsigned b, float c) {
  return __builtin_amdgcn_fdot2_f32_bf16(__builtin_bit_cast(bf16x2, a), __builtin_bit_cast(bf16x2, b), c, false);
}
template <int CTRL, int BANK> __device__ __forceinline__ unsigned dpp(unsigned old, unsigned src) {
  return __builtin_amdgcn_update_dpp(old, src, CTRL, 0xf, BANK, false);
}
template <int CTRL> __device__ __forceinline__ float dppf(float v) {
  return __uint_as_float(__builtin_amdgcn_update_dpp(0u, __float_as_uint(v), CTRL, 0xf, 0xf, true));
}
__device__ __forceinline__ v4u ld(rsrc_t r, unsigned off) { return __builtin_amdgcn_raw_buffer_load_b128(r, off, 0, 0); }
__device__ __forceinline__ v4u ldc(rsrc_t r, unsigned off) { return __builtin_amdgcn_raw_buffer_load_b128(r, off, 0, SC1); }
__device__ __forceinline__ void stc(rsrc_t r, unsigned off, v4u v) { __builtin_amdgcn_raw_buffer_store_b128(v, r, off, 0, SC1); }
__device__ __forceinline__ rsrc_t mkrs(const void* p, unsigned n) { return __builtin_amdgcn_make_buffer_rsrc((void*)p, 0, n, 0x00020000); }
template <int AUX> __device__ __forceinline__ void dma(v4u srd, LAS unsigned char* dst, unsigned voff, unsigned soff) {
  const unsigned d = (unsigned)(uintptr_t)dst;
  if (AUX == 0) asm volatile("s_mov_b32 m0, %0\n\ts_nop 0\n\tbuffer_load_dwordx4 %1, %2, %3 offen lds" ::"s"(d), "v"(voff), "s"(srd), "s"(soff) : "memory");
  else asm volatile("s_mov_b32 m0, %0\n\ts_nop 0\n\tbuffer_load_dwordx4 %1, %2, %3 offen sc1 lds" ::"s"(d), "v"(voff), "s"(srd), "s"(soff) : "memory");
}
__device__ __forceinline__ v4u mksrd(const void* p, unsigned n) {
  const unsigned long long a = (unsigned long long)p;
  return v4u{(unsigned)a, (unsigned)(a >> 32) & 0xffffu, n, 0x00020000u};
}
__device__ __forceinline__ v4u lds_(rsrc_t r, unsigned voff, unsigned soff) { return __builtin_amdgcn_raw_buffer_load_b128(r, voff, soff, 0); }
__device__ __forceinline__ v4u ldcs(rsrc_t r, unsigned voff, unsigned soff) { return __builtin_amdgcn_raw_buffer_load_b128(r, voff, soff, SC1); }
__device__ __forceinline__ v4u trfrag(LAS unsigned char* a, int hi_off) {
  const v2u lo = __builtin_bit_cast(v2u, __builtin_amdgcn_ds_read_tr16_b64_v4i16((LAS s16x4*)a));
  const v2u hi = __builtin_bit_cast(v2u, __builtin_amdgcn_ds_read_tr16_b64_v4i16((LAS s16x4*)(a + hi_off)));
  return v4u{lo.x, lo.y, hi.x, hi.y};
}
#define WAITV(n) asm volatile("s_waitcnt vmcnt(" #n ")" ::: "memory")
#define PIN(x) asm volatile("" : "+v"(x))
#define SBAR() __builtin_amdgcn_sched_barrier(0)
#define LBAR() do { asm volatile("s_waitcnt lgkmcnt(0)" ::: "memory"); __builtin_amdgcn_s_barrier(); asm volatile("" ::: "memory"); } while (0)
#define DRAIN() asm volatile("s_waitcnt vmcnt(0)" ::: "memory")
constexpr unsigned PLO = 0x05040100u, PHI = 0x07060302u;
__device__ __forceinline__ unsigned prm(unsigned hi, unsigned lo, unsigned sel) { return __builtin_amdgcn_perm(hi, lo, sel); }

__device__ __forceinline__ void xform64(const v4u (&X)[4], v4u (&F)[4]) {
  unsigned nx[4], ny[4], nz[4], nw[4];
#pragma unroll
  for (int i = 0; i < 4; ++i) {
    nz[i] = dpp<0x128, 0x3>(X[i].z, X[i].x);
    nw[i] = dpp<0x128, 0x3>(X[i].w, X[i].y);
    nx[i] = dpp<0x128, 0xC>(X[i].x, X[i].z);
    ny[i] = dpp<0x128, 0xC>(X[i].y, X[i].w);
  }
  F[0] = v4u{prm(nx[1], nx[0], PLO), prm(nx[3], nx[2], PLO), prm(nz[1], nz[0], PLO), prm(nz[3], nz[2], PLO)};
  F[1] = v4u{prm(nx[1], nx[0], PHI), prm(nx[3], nx[2], PHI), prm(nz[1], nz[0], PHI), prm(nz[3], nz[2], PHI)};
  F[2] = v4u{prm(ny[1], ny[0], PLO), prm(ny[3], ny[2], PLO), prm(nw[1], nw[0], PLO), prm(nw[3], nw[2], PLO)};
  F[3] = v4u{prm(ny[1], ny[0], PHI), prm(ny[3], ny[2], PHI), prm(nw[1], nw[0], PHI), prm(nw[3], nw[2], PHI)};
}
__device__ __forceinline__ void xform32(v4u (&X)[2]) {
  unsigned ax[2], ay[2], az[2], aw[2];
#pragma unroll
  for (int i = 0; i < 2; ++i) {
    const unsigned nz = dpp<0x128, 0x3>(X[i].z, X[i].x), nw = dpp<0x128, 0x3>(X[i].w, X[i].y);
    const unsigned nx = dpp<0x128, 0xC>(X[i].x, X[i].z), ny = dpp<0x128, 0xC>(X[i].y, X[i].w);
    ay[i] = dpp<0x12C, 0x5>(ny, nx);
    aw[i] = dpp<0x12C, 0x5>(nw, nz);
    ax[i] = dpp<0x124, 0xA>(nx, ny);
    az[i] = dpp<0x124, 0xA>(nz, nw);
  }
  X[0] = v4u{prm(ax[1], ax[0], PLO), prm(ay[1], ay[0], PLO), prm(az[1], az[0], PLO), prm(aw[1], aw[0], PLO)};
  X[1] = v4u{prm(ax[1], ax[0], PHI), prm(ay[1], ay[0], PHI), prm(az[1], az[0], PHI), prm(aw[1], aw[0], PHI)};
}
__device__ __forceinline__ float xmax16_32(float v) {
  auto a = __builtin_amdgcn_permlane16_swap(__float_as_uint(v), __float_as_uint(v), false, false);
  v = fmaxf(__uint_as_float(a[0]), __uint_as_float(a[1]));
  auto b = __builtin_amdgcn_permlane32_swap(__float_as_uint(v), __float_as_uint(v), false, false);
  return fmaxf(__uint_as_float(b[0]), __uint_as_float(b[1]));
}
__device__ __forceinline__ float xsum16_32(float v) {
  auto a = __builtin_amdgcn_permlane16_swap(__float_as_uint(v), __float_as_uint(v), false, false);
  v = __uint_as_float(a[0]) + __uint_as_float(a[1]);
  auto b = __builtin_amdgcn_permlane32_swap(__float_as_uint(v), __float_as_uint(v), false, false);
  return __uint_as_float(b[0]) + __uint_as_float(b[1]);
}

__device__ __forceinline__ void poll(unsigned char* ws, unsigned off, unsigned long long tag) {
  gu64* p = (gu64*)(ws + off);
#pragma unroll 1
  for (unsigned spin = 0; spin < (1u << 17); ++spin) {
    const unsigned long long v = __hip_atomic_load(p, RLX_AGENT);
    if (__all(off == 0 || v == tag)) break;
    __builtin_amdgcn_s_sleep(2);
  }
  asm volatile("" ::: "memory");
}
__device__ __forceinline__ void post(rsrc_t rWS, unsigned off, unsigned long long tag, int lane) {
  if (lane < 8) stc(rWS, off + lane * 16u, v4u{(unsigned)tag, (unsigned)(tag >> 32), (unsigned)tag, (unsigned)(tag >> 32)});
}

#define STAMP(k) do {} while (0)

#define CPF 1
#define WAITPF(n) WAITV(n)
#define PRO 2
#define ROT 0
#define TOUCH 1
#define P1_UNROLL 1
constexpr int W1_EARLY = 4, W1_MID = 10;
struct Args { const unsigned char *x, *wqkv, *wo, *w1, *w2; unsigned char *out, *ws; };
__device__ __forceinline__ float max3(float a, float b, float c) { return fmaxf(fmaxf(a, b), c); }
#define BAR() do { __builtin_amdgcn_s_barrier(); asm volatile("" ::: "memory"); } while (0)
#define WAITL() asm volatile("s_waitcnt lgkmcnt(0)" ::: "memory")

__global__ void __launch_bounds__(512, 2) fused_block(Args A) {
  __shared__ __attribute__((aligned(16))) unsigned char smem[L_TOTAL];
  LAS unsigned char* L = (LAS unsigned char*)smem;
  unsigned char* ws = A.ws;
  const int tid = threadIdx.x, lane = tid & 63;
  const int w = __builtin_amdgcn_readfirstlane(tid >> 6);
  const int bid = blockIdx.x, xcd = bid & 7, slot = bid >> 3;
  const bool gemm = slot < 28;
  const int rb = 4 * xcd + slot / 7, j = slot % 7;
  const int hd = 2 * xcd + (slot >> 4), ab = slot & 15;
  const int tok = lane & 15, g = lane >> 4;
  const v4u ones = {0x3f803f80u, 0x3f803f80u, 0x3f803f80u, 0x3f803f80u};
  STAMP(0);
  unsigned long long pc0;
  unsigned clen;
  asm volatile("s_getpc_b64 %0\n.Lfb_here:\n\ts_mov_b32 %1, .Lfb_end-.Lfb_here" : "=s"(pc0), "=s"(clen));
  auto cpf = [&]() {
    const unsigned o = (unsigned)(slot * 16 + lane) * 128u;
    if (w == 3 && lane < 16 && o < clen) { const unsigned v = *(const GAS unsigned*)((pc0 & ~3ull) + o); asm volatile("" ::"v"(v)); }
  };
  cpf();
  const v4u dX = mksrd(A.x, SZ_ACT), dWQ = mksrd(A.wqkv, 448u * 2688u), dWO = mksrd(A.wo, 448u * 896u), dWS = mksrd(ws, WS_END);
  const rsrc_t rWO = mkrs(A.wo, 448u * 896u);
  const rsrc_t rW1 = mkrs(A.w1, 448u * 3584u), rW2 = mkrs(A.w2, 1792u * 896u), rWS = mkrs(ws, WS_END), rOUT = mkrs(A.out, SZ_ACT);
  const int row8 = tid >> 3, c8 = tid & 7;
  const unsigned long long tag = *(const __attribute__((address_space(4))) unsigned long long*)(ws + WS_EPOCH) + 1ull;

  v4u w1f[14][2];
  const unsigned w1v = (unsigned)(16 * ((lane >> 3) & 1) + 4 * g + 2 * ((lane >> 2) & 1)) * 3584u + (unsigned)(32 * w + 8 * (lane & 3)) * 2u;
  auto ldw1 = [&](int k) {
    int kg = 2 * j + k; kg = kg >= 14 ? kg - 14 : kg;
#pragma unroll
    for (int i = 0; i < 2; ++i) w1f[k][i] = lds_(rW1, w1v + i * 3584u, (unsigned)kg * (32u * 3584u) + j * 512u);
  };

  if (gemm) {
    const int q4 = (lane >> 2) & 3, p4 = lane & 3, qt = w & 3, cs = qt & 1;
    const int fr = (q4 >> 1) | ((g & 1) << 1);
    const int trow = 8192 + (8 * g + q4) * 128 + (p4 >> 1) * 16 + (p4 & 1) * 8;
    const int ta0 = trow + (qt >> 1) * 8192 + (((2 * cs) ^ fr) * 32);
    const int ta1 = trow + (qt >> 1) * 8192 + (((2 * cs + 1) ^ fr) * 32);
    const int tav = trow + 2 * 8192 + ((qt ^ fr) * 32);
    if (w >= 4) {
      int rot = ROT == 0 ? 0 : ROT == 1 ? j : j + 2 * (slot / 7); rot = rot >= 7 ? rot - 7 : rot;
      const int r0 = 16 * qt + (lane >> 3);
      const unsigned xv0 = (unsigned)(rb * 64 + r0) * 896u + (unsigned)(c8 ^ ((r0 >> 1) & 7)) * 16u;
      const unsigned xv1 = (unsigned)(rb * 64 + r0 + 8) * 896u + (unsigned)(c8 ^ (((r0 >> 1) + 4) & 7)) * 16u;
      const unsigned wv0 = (unsigned)r0 * 2688u + j * 128u + (unsigned)(c8 ^ (r0 & 2)) * 16u;
      const unsigned wv1 = (unsigned)(r0 + 8) * 2688u + j * 128u + (unsigned)(c8 ^ ((r0 & 2) | 4)) * 16u;
      auto issue = [&](int p) {
        if (p < 7) {
          int pa = p + rot; pa = pa >= 7 ? pa - 7 : pa;
          LAS unsigned char* d = L + L_RING + ((p + 1) & 3) * 32768 + qt * 2048;
          dma<0>(dX, d, xv0, pa * 128u); dma<0>(dX, d + 1024, xv1, pa * 128u);
#pragma unroll
          for (int s = 0; s < 3; ++s) {
            dma<0>(dWQ, d + 8192 + s * 8192, wv0, pa * (64u * 2688u) + s * 896u);
            dma<0>(dWQ, d + 9216 + s * 8192, wv1, pa * (64u * 2688u) + s * 896u);
          }
        } else if (p < 9) {
#pragma unroll
          for (int i = 0; i < 8; ++i) {
            const int d = 8 * (i & 3) + (lane >> 3), hh = 8 * (p - 7) + 2 * qt + (i >> 2);
            dma<0>(dWO, L + L_WO + hh * 4096 + (i & 3) * 1024, (unsigned)(d < 28 ? d : 27) * 896u + (unsigned)(c8 ^ (((d >> 1) & 3) << 1)) * 16u,
                   (unsigned)(28 * hh) * 896u + j * 128u);
          }
        }
      };
      __builtin_amdgcn_s_setprio(3);
      issue(0); issue(1);
      if (PRO >= 3) issue(2);
      if (PRO >= 4) issue(3);
      if (PRO == 2) WAITV(8); else if (PRO == 3) WAITV(16); else WAITV(24);
      BAR();
      STAMP(1);
#pragma unroll
      for (int p = 0; p < 7; ++p) {
        if (p == 0 && PRO < 3) issue(2);
        if ((p >= 1 || PRO < 4) && p + 3 < 7) issue(p + 3);
        int pa = p + rot; pa = pa >= 7 ? pa - 7 : pa;
        if (pa == j) {
          LAS unsigned char* sl = L + L_RING + ((p + 1) & 3) * 32768;
          const v4u xa = *(LAS v4u*)(sl + qt * 2048 + lane * 16), xb = *(LAS v4u*)(sl + qt * 2048 + 1024 + lane * 16);
          *(LAS v4u*)(L + L_XS + r0 * 144 + (c8 ^ ((r0 >> 1) & 7)) * 16) = xa;
          *(LAS v4u*)(L + L_XS + (r0 + 8) * 144 + (c8 ^ (((r0 >> 1) + 4) & 7)) * 16) = xb;
        }
        if (p < 6) { WAITL(); if (p < 4) WAITV(16); else if (p == 4) WAITV(8); else WAITV(0); BAR(); }
      }
      __builtin_amdgcn_s_setprio(0);
      LBAR();
      STAMP(2);
      BAR();
      issue(7); issue(8);
#pragma unroll
      for (int k = 0; k < W1_EARLY; ++k) ldw1(k);
      SBAR();
    } else {
      int bx[2][2], bs[2][2], bw[2][3];
#pragma unroll
      for (int h = 0; h < 2; ++h) {
        const int x0 = tok * 128 + ((g ^ (tok >> 1)) * 16) + h * 65536;
        bx[h][0] = x0; bx[h][1] = x0 ^ 64; bs[h][0] = x0 + qt * 2048; bs[h][1] = (x0 ^ 64) + qt * 2048;
        bw[h][0] = ta0 + h * 65536; bw[h][1] = ta1 + h * 65536; bw[h][2] = tav + h * 65536 - 8192;
        PIN(bx[h][0]); PIN(bx[h][1]); PIN(bs[h][0]); PIN(bs[h][1]); PIN(bw[h][0]); PIN(bw[h][1]); PIN(bw[h][2]);
      }
      f32x4 aq[4][2], av[4], c0 = {0, 0, 0, 0}, c1 = {0, 0, 0, 0}, cv = {0, 0, 0, 0}, rs1 = {0, 0, 0, 0}, rs2 = {0, 0, 0, 0};
#pragma unroll
      for (int m = 0; m < 4; ++m) { aq[m][0] = f32x4{0, 0, 0, 0}; aq[m][1] = f32x4{0, 0, 0, 0}; av[m] = f32x4{0, 0, 0, 0}; }
      v2u fw[2][3][2];
      v4u fx[2][5];
      auto rd1 = [&](int p, int kk, int i) {
        const int sl = (p + 1) & 3, h = sl >> 1, so = (sl & 1) * 32768;
        if (i < 6) fw[kk][i >> 1][i & 1] = __builtin_bit_cast(v2u, __builtin_amdgcn_ds_read_tr16_b64_v4i16(
                       (LAS s16x4*)(L + bw[h][i >> 1] + so + kk * 4096 + (i & 1) * 512 + (i >= 4 ? 8192 : 0))));
        else if (i < 10) fx[kk][i - 6] = *(LAS v4u*)(L + bx[h][kk] + so + (i - 6) * 2048);
        else fx[kk][4] = *(LAS v4u*)(L + bs[h][kk] + so);
      };
      auto mm = [&](int kk, int pn) {
        const v4u f0 = {fw[kk][0][0].x, fw[kk][0][0].y, fw[kk][0][1].x, fw[kk][0][1].y};
        const v4u f1 = {fw[kk][1][0].x, fw[kk][1][0].y, fw[kk][1][1].x, fw[kk][1][1].y};
        const v4u f2 = {fw[kk][2][0].x, fw[kk][2][0].y, fw[kk][2][1].x, fw[kk][2][1].y};
        int n = 0;
        auto nx = [&]() { if (pn < 7 && n < 11) rd1(pn, kk ^ 1, n); ++n; SBAR(); };
        c0 = mfma(f0, ones, c0); nx();
        c1 = mfma(f1, ones, c1); nx();
        cv = mfma(ones, f2, cv); nx();
#pragma unroll
        for (int m = 0; m < 4; ++m) {
          aq[m][0] = mfma(f0, fx[kk][m], aq[m][0]); nx();
          aq[m][1] = mfma(f1, fx[kk][m], aq[m][1]); nx();
          av[m] = mfma(fx[kk][m], f2, av[m]); nx();
        }
        rs1 = mfma(ones, fx[kk][4], rs1); nx();
        rs2 = mfma(fx[kk][4], fx[kk][4], rs2); nx();
      };
      {
        const rsrc_t rWQ = mkrs(A.wqkv, 448u * 2688u), rX = mkrs(A.x, SZ_ACT);
        unsigned tv = 0;
        if (w == 0) tv = __builtin_amdgcn_raw_buffer_load_b32(rWQ, lane < 42 ? (unsigned)((rb * 7 + j) * 42 + lane) * 128u : OOB, 0, 0);
        if (w == 1) tv = __builtin_amdgcn_raw_buffer_load_b32(rX, (unsigned)((rb * 7 + j) * 64 + lane) * 128u, 0, 0);
        asm volatile("" ::"v"(tv));
      }
      if (tid == 0) *(LAS unsigned*)(L + L_CNT) = 0u;
      BAR();
      STAMP(1);
#pragma unroll
      for (int i = 0; i < 11; ++i) rd1(0, 0, i);
      SBAR();
#pragma unroll
      for (int p = 0; p < 7; ++p) {
        mm(0, p);
        if (p < 5) { WAITL(); BAR(); mm(1, p + 1); }
        else if (p == 5) {
          mm(1, 7);
          BAR();
#pragma unroll
          for (int i = 0; i < 11; ++i) rd1(6, 0, i);
          SBAR();
        } else mm(1, 7);
      }
      {
        const float d01 = (tok & 1) ? rs2[1] : rs2[0], d23 = (tok & 1) ? rs2[3] : rs2[2], dd = (tok & 2) ? d23 : d01;
        const float mean = rs1[0] * (1.0f / 448.0f);
        const float rstd = __builtin_amdgcn_rsqf(fmaxf(dd * (1.0f / 448.0f) - mean * mean, 0.f) + 1e-5f);
        if (g == (tok >> 2)) *(LAS f32x2*)(L + L_ST + (16 * qt + tok) * 8) = f32x2{mean, rstd};
      }
      LBAR();
      STAMP(2);
      const float cvs = cv[0];
      const float sc = (qt < 2) ? 0.18898223650461362f * 1.4426950408889634f : 1.0f;
      const unsigned qkb = ((qt < 2) ? WS_Q : WS_K) + (unsigned)(8 * j + 4 * cs + (((g & 1) << 1) | (g >> 1))) * 32768u + (unsigned)(rb * 64 + tok) * 16u;
      unsigned vv[4];
#pragma unroll
      for (int m = 0; m < 4; ++m) {
        const int ma = m;
        const f32x2 ms = *(LAS f32x2*)(L + L_ST + (16 * ma + tok) * 8);
        const float rs = ms.y * sc;
        const f32x4 a = (aq[m][0] - ms.x * c0) * rs, b = (aq[m][1] - ms.x * c1) * rs;
        auto t0 = __builtin_amdgcn_permlane16_swap(pk(a[0], a[1]), pk(b[0], b[1]), false, false);
        auto t1 = __builtin_amdgcn_permlane16_swap(pk(a[2], a[3]), pk(b[2], b[3]), false, false);
        stc(rWS, qkb + ma * 256u, v4u{t0[0], t1[0], t0[1], t1[1]});
        const f32x4 e0 = *(LAS f32x4*)(L + L_ST + (16 * ma + 4 * g) * 8), e1 = *(LAS f32x4*)(L + L_ST + (16 * ma + 4 * g) * 8 + 16);
        vv[2 * (m & 1)] = pk((av[m][0] - e0[0] * cvs) * e0[1], (av[m][1] - e0[2] * cvs) * e0[3]);
        vv[2 * (m & 1) + 1] = pk((av[m][2] - e1[0] * cvs) * e1[1], (av[m][3] - e1[2] * cvs) * e1[3]);
        if (m & 1)
          stc(rWS, WS_V + (unsigned)((2 * rb + (ma >> 1)) * 4 + g) * 7168u + (unsigned)(64 * j + 16 * qt + tok) * 16u,
              v4u{vv[0], vv[1], vv[2], vv[3]});
      }
      SBAR();
      BAR();
    }
  } else {
    {
      for (int i = 0; i < WARM_DELAY; ++i) __builtin_amdgcn_s_sleep(16);
      const unsigned t = (unsigned)((xcd * 4 + slot - 28) * 512 + tid) * 128u;
      const unsigned a0 = __builtin_amdgcn_raw_buffer_load_b32(rWO, t, 0, 0), a1 = __builtin_amdgcn_raw_buffer_load_b32(rW1, t, 0, 0),
                     a2 = __builtin_amdgcn_raw_buffer_load_b32(rW2, t, 0, 0);
      asm volatile("" ::"v"(a0), "v"(a1), "v"(a2));
    }
  }

  {
    f32x4 s[18];
    const int lg0 = w >> 1;
    {
      const int kmin = (ab < 2) ? (256 - 128 * ab) : 0;
      const int d0 = 16 * (w & 1) + tok - 4 * g;
#pragma unroll
      for (int t = 0; t < 18; ++t) {
        const float u = (32 * lg0 + 16 * t >= kmin) ? 0.f : -__builtin_inff();
#pragma unroll
        for (int r = 0; r < 4; ++r) {
          if (t < 2) s[t][r] = (d0 < 16 * t + r) ? u : -__builtin_inff();
          else if (t >= 16) s[t][r] = (d0 >= 16 * (t - 16) + r) ? 0.f : -__builtin_inff();
          else s[t][r] = u;
        }
        PIN(s[t]);
      }
    }
    if ((w & 1) && tok >= 12) {
#pragma unroll
      for (int i = 0; i < 3; ++i) *(LAS v4u*)(L + L_VI + (w + 8 * i) * 1024 + lane * 16) = ones;
    }
    SBAR();
    if (w < 4 || !gemm) {
      if (gemm) {
        DRAIN();
        unsigned old = 0;
        if (lane == 0) old = __hip_atomic_fetch_add((LAS unsigned*)(L + L_CNT), 1u, __ATOMIC_RELAXED, __HIP_MEMORY_SCOPE_WORKGROUP);
        if (__builtin_amdgcn_readfirstlane(old) == 3u) post(rWS, WS_F0 + (unsigned)(rb * 7 + j) * 128u, tag, lane);
      }
#pragma unroll
      for (int k = 0; k < W1_EARLY; ++k) ldw1(k);
    }
    SBAR();
    STAMP(3);
#pragma unroll
    for (int k = 0; k < 4; ++k) { xform32(w1f[k]); PIN(w1f[k][0]); PIN(w1f[k][1]); }
    SBAR();
    if (w >= 4) {
      const int j0 = (28 * hd) >> 6, j1 = (28 * hd + 27) >> 6;
      const int r_ = 2 * ab - 4 + (lane >> 1), jj = (lane & 1) ? j1 : j0;
      poll(ws, (lane < 12 && r_ >= 0) ? WS_F0 + (unsigned)(r_ * 7 + jj) * 128u : 0u, tag);
      STAMP(4);
      const int c0 = (7 * hd - (hd & 1)) >> 1, lw = w - 4;
      const unsigned kv = (unsigned)(c0 + g) * 32768u + tok * 16u;
      dma<SC1>(dWS, L + L_QI + lw * 2048, kv, WS_Q + (unsigned)(128 * ab + 32 * lw) * 16u);
      dma<SC1>(dWS, L + L_QI + lw * 2048 + 1024, kv, WS_Q + (unsigned)(128 * ab + 32 * lw + 16) * 16u);
#pragma unroll
      for (int i = 0; i < 6; ++i) {
        const int t0 = 128 * ab - 256 + 16 * (lw + 4 * i);
        dma<SC1>(dWS, L + L_KI + (lw + 4 * i) * 1024, t0 >= 0 ? kv : OOB, WS_K + (unsigned)(t0 >= 0 ? t0 : 0) * 16u);
      }
      if (!((w & 1) && tok >= 12)) {
#pragma unroll
        for (int i = 0; i < 6; ++i) {
          const int tg = 4 * ab - 8 + ((lw + 4 * i) >> 1);
          dma<SC1>(dWS, L + L_VI + (lw + 4 * i) * 1024, tg >= 0 ? (unsigned)g * 7168u + (unsigned)(28 * hd + 16 * (w & 1) + tok) * 16u : OOB,
                   WS_V + (unsigned)(tg >= 0 ? tg : 0) * 28672u);
        }
      }
    } else { STAMP(4); }
    SBAR();
    WAITV(6);
    LBAR();
    __builtin_amdgcn_s_setprio(2);
    STAMP(5);
    v4u qf = *(LAS v4u*)(L + L_QI + w * 1024 + lane * 16);
    {
      const bool zlo = (hd & 1) && g == 0, zhi = !(hd & 1) && g == 3;
      qf.x = zlo ? 0u : qf.x; qf.y = zlo ? 0u : qf.y; qf.z = zhi ? 0u : qf.z; qf.w = zhi ? 0u : qf.w;
    }
    int kb = L_KI + lg0 * 2048 + lane * 16;
    PIN(kb);
    float mx0 = -__builtin_inff(), mx1 = -__builtin_inff();
    {
      v4u kf[6];
#pragma unroll
      for (int t = 0; t < 6; ++t) kf[t] = *(LAS v4u*)(L + kb + t * 1024);
      SBAR();
#pragma unroll
      for (int t = 0; t < 18; ++t) {
        s[t] = mfma(kf[t % 6], qf, s[t]);
        if (t + 6 < 18) kf[t % 6] = *(LAS v4u*)(L + kb + (t + 6) * 1024);
        if (t >= 2) { mx0 = max3(mx0, s[t - 2][0], s[t - 2][1]); mx1 = max3(mx1, s[t - 2][2], s[t - 2][3]); }
        SBAR();
      }
    }
    DRAIN();
    BAR();
    v4u vf[2][2];
    vf[0][0] = *(LAS v4u*)(L + kb + 24576); vf[0][1] = *(LAS v4u*)(L + kb + 24576 + 1024);
#pragma unroll
    for (int t = 16; t < 18; ++t) { mx0 = max3(mx0, s[t][0], s[t][1]); mx1 = max3(mx1, s[t][2], s[t][3]); }
    const float mx = xmax16_32(fmaxf(mx0, mx1));
    f32x4 o0 = {0, 0, 0, 0}, o1 = {0, 0, 0, 0};
    SBAR();
#pragma unroll
    for (int gi = 0; gi < 9; ++gi) {
      if (gi < 8) {
        vf[(gi + 1) & 1][0] = *(LAS v4u*)(L + kb + 24576 + (gi + 1) * 2048);
        vf[(gi + 1) & 1][1] = *(LAS v4u*)(L + kb + 24576 + (gi + 1) * 2048 + 1024);
        if (W1_EARLY + gi < W1_MID) ldw1(W1_EARLY + gi);
      }
      float e[8];
#pragma unroll
      for (int r = 0; r < 4; ++r) { e[r] = __builtin_amdgcn_exp2f(s[2 * gi][r] - mx); e[4 + r] = __builtin_amdgcn_exp2f(s[2 * gi + 1][r] - mx); }
      const v4u pf = {pk(e[0], e[1]), pk(e[2], e[3]), pk(e[4], e[5]), pk(e[6], e[7])};
      o0 = mfma(vf[gi & 1][0], pf, o0);
      o1 = mfma(vf[gi & 1][1], pf, o1);
      SBAR();
    }
    const float den = __shfl(o1[0], tok + 48);
    const float inv = __builtin_amdgcn_rcpf(den);
    o0 *= inv; o1 *= inv;
    v4u ov = {pk(o0[0], o0[1]), pk(o0[2], o0[3]), pk(o1[0], o1[1]), pk(o1[2], o1[3])};
    if (g == 3) { ov.z = 0; ov.w = 0; }
    STAMP(6);
    stc(rWS, WS_O + (unsigned)(((2 * ab + (w >> 2)) * 16 + hd) * 4 + (w & 3)) * 1024u + lane * 16u, ov);
    __builtin_amdgcn_s_setprio(0);
    DRAIN();
    post(rWS, WS_F1 + (unsigned)((8 * ab + w) * 16 + hd) * 128u, tag, lane);
    STAMP(7);
  }
  if (!gemm) {
    if (bid == 255 && w == 0) {
#pragma unroll 1
      for (int q = 0; q < 4; ++q) poll(ws, WS_F1 + (unsigned)((4 * q + g) * 128 + tok) * 128u, tag);
      if (lane == 0) __hip_atomic_store((gu64*)(ws + WS_EPOCH), tag, RLX_AGENT);
    }
    return;
  }

  v4u w2r[7][4];
  const unsigned w2v = (unsigned)(32 * w + 2 * g + ((lane >> 3) & 1)) * 896u + (lane & 7) * 16u;
  auto ldw2 = [&](int s) {
    int sg = j + s; sg = sg >= 7 ? sg - 7 : sg;
#pragma unroll
    for (int i = 0; i < 4; ++i) w2r[s][i] = lds_(rW2, w2v, (unsigned)(256 * sg + 8 * i) * 896u + j * 128u);
  };
  auto ldw2i = [&](int s, int i) {
    int sg = j + s; sg = sg >= 7 ? sg - 7 : sg;
    w2r[s][i] = lds_(rW2, w2v, (unsigned)(256 * sg + 8 * i) * 896u + j * 128u);
    SBAR();
  };
  auto xfw2 = [&](int s) {
    v4u F[4]; xform64(w2r[s], F);
#pragma unroll
    for (int n = 0; n < 4; ++n) { w2r[s][n] = F[n]; PIN(w2r[s][n]); }
  };
  ldw1(W1_MID); ldw1(W1_MID + 1);
  SBAR();
#pragma unroll
    for (int k = 4; k < 7; ++k) { xform32(w1f[k]); PIN(w1f[k][0]); PIN(w1f[k][1]); }
    SBAR();

  {
    const int tt = w & 3, kh = w >> 2;
    const int q4 = (lane >> 2) & 3, p4 = lane & 3;
    const int fo = (2 * g + (q4 >> 1)) & 3;
    const int wb = L_WO + kh * 32768 + (4 * g + q4) * 128 + (p4 >> 1) * 16 + (p4 & 1) * 8;
    v4u wf[2][4];
    auto rdw = [&](int i) {
#pragma unroll
      for (int n = 0; n < 4; ++n) wf[i & 1][n] = trfrag(L + wb + ((n ^ fo) * 32) + i * 4096, 2048);
    };
    rdw(0); rdw(1);
    SBAR();
    poll(ws, lane < 8 ? WS_F1 + (unsigned)((rb * 4 + tt) * 16 + 8 * kh + lane) * 128u : 0u, tag);
    STAMP(8);
    if (CPF >= 3) cpf();
    v4u of[8];
#pragma unroll
    for (int i = 0; i < 8; ++i) of[i] = ldcs(rWS, lane * 16u, WS_O + (unsigned)((rb * 16 + 8 * kh + i) * 4 + tt) * 1024u);
    SBAR();
    ldw1(W1_MID + 2); ldw1(W1_MID + 3);
    SBAR();
#pragma unroll
    for (int k = 7; k < 9; ++k) { xform32(w1f[k]); PIN(w1f[k][0]); PIN(w1f[k][1]); }
    SBAR();
    __builtin_amdgcn_s_setprio(2);
    f32x4 a[4];
#pragma unroll
    for (int n = 0; n < 4; ++n) a[n] = f32x4{0, 0, 0, 0};
#pragma unroll
    for (int i = 0; i < 8; ++i) {
#pragma unroll
      for (int n = 0; n < 4; ++n) a[n] = mfma(wf[i & 1][n], of[i], a[n]);
      SBAR();
      if (i + 2 < 8) rdw(i + 2);
      SBAR();
    }
    STAMP(9);
    const f32x4 s0 = kh ? a[0] : a[2], s1 = kh ? a[1] : a[3];
    f32x4 k0 = kh ? a[2] : a[0], k1 = kh ? a[3] : a[1];
    const int rmine = (kh ? L_RED1 : L_RED0) + tt * 2048 + lane * 16, rother = (kh ? L_RED0 : L_RED1) + tt * 2048 + lane * 16;
    *(LAS f32x4*)(L + rmine) = s0;
    *(LAS f32x4*)(L + rmine + 1024) = s1;
    const int so = (16 * tt + tok) * 144 + (32 * kh + 4 * g) * 2;
    const v2u xa = *(LAS v2u*)(L + L_XS + so), xb = *(LAS v2u*)(L + L_XS + so + 32);
    LBAR();
    k0 += *(LAS f32x4*)(L + rother);
    k1 += *(LAS f32x4*)(L + rother + 1024);
    const v4u hv = {pk(blo(xa.x) + k0[0], bhi(xa.x) + k0[1]), pk(blo(xa.y) + k0[2], bhi(xa.y) + k0[3]),
                    pk(blo(xb.x) + k1[0], bhi(xb.x) + k1[1]), pk(blo(xb.y) + k1[2], bhi(xb.y) + k1[3])};
    stc(rWS, WS_H + (unsigned)((rb * 14 + 2 * j + kh) * 4 + tt) * 1024u + lane * 16u, hv);
    SBAR();
    {
      const f32x4 r1 = mfma(ones, hv, f32x4{0, 0, 0, 0}), r2 = mfma(hv, hv, f32x4{0, 0, 0, 0});
      const float d01 = (tok & 1) ? r2[1] : r2[0], d23 = (tok & 1) ? r2[3] : r2[2], dd = (tok & 2) ? d23 : d01;
      __builtin_amdgcn_raw_buffer_store_b64(v2u{__float_as_uint(r1[0]), __float_as_uint(dd)}, rWS,
          g == (tok >> 2) ? WS_HS + (unsigned)((rb * 8 + w) * 8 + j) * 128u + tok * 8u : OOB, 0, SC1);
    }
    __builtin_amdgcn_s_setprio(0);
    SBAR();
    *(LAS v2u*)(L + L_HS + so) = v2u{hv.x, hv.y};
    *(LAS v2u*)(L + L_HS + so + 32) = v2u{hv.z, hv.w};
    *(LAS v4u*)(L + L_HI + (kh * 4 + tt) * 1024 + lane * 16) = hv;
    SBAR();
#pragma unroll
    for (int k = 9; k < 11; ++k) { xform32(w1f[k]); PIN(w1f[k][0]); PIN(w1f[k][1]); }
    SBAR();
    LBAR();
    DRAIN();
    post(rWS, WS_F2 + (unsigned)((rb * 8 + w) * 8 + j) * 128u, tag, lane);
    STAMP(10);
  }

  v4u ff[4];
  {
    ldw2(0);
    SBAR();
    int hb = L_HI + lane * 16;
    PIN(hb);
    f32x4 a[4][2], cs0 = {0, 0, 0, 0}, cs1 = {0, 0, 0, 0};
#pragma unroll
    for (int m = 0; m < 4; ++m) {
      const v4u h0 = *(LAS v4u*)(L + hb + m * 1024), h1 = *(LAS v4u*)(L + hb + (4 + m) * 1024);
      a[m][0] = mfma(w1f[0][0], h0, f32x4{0, 0, 0, 0}); a[m][1] = mfma(w1f[0][1], h0, f32x4{0, 0, 0, 0});
      a[m][0] = mfma(w1f[1][0], h1, a[m][0]); a[m][1] = mfma(w1f[1][1], h1, a[m][1]);
    }
    cs0 = mfma(w1f[0][0], ones, cs0); cs1 = mfma(w1f[0][1], ones, cs1);
    cs0 = mfma(w1f[1][0], ones, cs0); cs1 = mfma(w1f[1][1], ones, cs1);
    SBAR();
#pragma unroll
    for (int k = 11; k < 13; ++k) { xform32(w1f[k]); PIN(w1f[k][0]); PIN(w1f[k][1]); }
    SBAR();
    poll(ws, (lane < 7 && lane != j) ? WS_F2 + (unsigned)((rb * 8 + w) * 8 + lane) * 128u : 0u, tag);
    STAMP(11);
    dma<SC1>(dWS, L + L_RED0 + w * 1024, lane < 56 ? lane * 16u : OOB, WS_HS + (unsigned)(rb * 8 + w) * 1024u);
#pragma unroll
    for (int i = 0; i < 6; ++i) {
      int kg = 2 * j + 2 + 2 * i; kg = kg >= 14 ? kg - 14 : kg;
      dma<SC1>(dWS, L + L_HI + (8 + 8 * i + w) * 1024, lane * 16u, WS_H + (unsigned)(rb * 56 + kg * 4 + w) * 1024u);
    }
    SBAR();
#pragma unroll
    for (int k = 13; k < 14; ++k) { xform32(w1f[k]); PIN(w1f[k][0]); PIN(w1f[k][1]); }
    SBAR();
#pragma unroll
    for (int k = 2; k < 14; ++k) { cs0 = mfma(w1f[k][0], ones, cs0); cs1 = mfma(w1f[k][1], ones, cs1); }
    xfw2(0);
    SBAR();
    WAITV(3);
    BAR();
    __builtin_amdgcn_s_setprio(2);
    STAMP(12);
    auto epi = [&](int m) {
      const f32x2 p0 = *(LAS f32x2*)(L + L_ST + (16 * m + tok) * 8), p1 = *(LAS f32x2*)(L + L_ST + (64 + 16 * m + tok) * 8);
      const float mean = (p0.x + p1.x) * (1.0f / 448.0f);
      const float rstd = __builtin_amdgcn_rsqf(fmaxf((p0.y + p1.y) * (1.0f / 448.0f) - mean * mean, 0.f) + 1e-5f);
      float f0[4], f1[4];
#pragma unroll
      for (int r = 0; r < 4; ++r) {
        f0[r] = fmaxf(fmaf(-mean, cs0[r], a[m][0][r]) * rstd, 0.f);
        f1[r] = fmaxf(fmaf(-mean, cs1[r], a[m][1][r]) * rstd, 0.f);
      }
      ff[m] = v4u{pk(f0[0], f0[1]), pk(f0[2], f0[3]), pk(f1[0], f1[1]), pk(f1[2], f1[3])};
      stc(rWS, WS_F + (unsigned)((rb * 56 + 8 * j + w) * 4 + m) * 1024u + lane * 16u, ff[m]);
    };
    v4u hf[2][4];
    auto rdh = [&](int u) {
      const int mh = 2 * (u / 6), k = 2 + 2 * (u % 6);
      hf[u & 1][0] = *(LAS v4u*)(L + hb + (k * 4 + mh) * 1024); hf[u & 1][1] = *(LAS v4u*)(L + hb + (k * 4 + mh + 1) * 1024);
      hf[u & 1][2] = *(LAS v4u*)(L + hb + (k * 4 + 4 + mh) * 1024); hf[u & 1][3] = *(LAS v4u*)(L + hb + (k * 4 + 5 + mh) * 1024);
    };
    rdh(0);
    SBAR();
#pragma unroll
    for (int u = 0; u < 12; ++u) {
      if (u == 2) { DRAIN(); BAR(); }
      if (u + 1 < 12) rdh(u + 1);
      SBAR();
      const int mh = 2 * (u / 6), k = 2 + 2 * (u % 6);
      const int ls = u >= 2 ? u / 2 : -1, li = 2 * (u & 1);
      a[mh][0] = mfma(w1f[k][0], hf[u & 1][0], a[mh][0]); a[mh][1] = mfma(w1f[k][1], hf[u & 1][0], a[mh][1]);
      a[mh + 1][0] = mfma(w1f[k][0], hf[u & 1][1], a[mh + 1][0]); a[mh + 1][1] = mfma(w1f[k][1], hf[u & 1][1], a[mh + 1][1]);
      if (u == 6) epi(0);
      SBAR();
      if (ls >= 0 && u != 6) ldw2i(ls, li);
      a[mh][0] = mfma(w1f[k + 1][0], hf[u & 1][2], a[mh][0]); a[mh][1] = mfma(w1f[k + 1][1], hf[u & 1][2], a[mh][1]);
      a[mh + 1][0] = mfma(w1f[k + 1][0], hf[u & 1][3], a[mh + 1][0]); a[mh + 1][1] = mfma(w1f[k + 1][1], hf[u & 1][3], a[mh + 1][1]);
      if (u == 0) {
        f32x4 t = *(LAS f32x4*)(L + L_RED0 + w * 1024 + lane * 16);
#pragma unroll
        for (int e = 0; e < 4; ++e) { t[e] += dppf<0x128>(t[e]); t[e] = xsum16_32(t[e]); }
        if (lane < 8) *(LAS f32x4*)(L + L_ST + ((w >> 2) * 64 + 16 * (w & 3)) * 8 + lane * 16) = t;
      }
      if (u == 1) WAITL();
      if (u == 6) epi(1);
      SBAR();
      if (u == 6) ldw2i(ls, li);
      if (ls >= 0) ldw2i(ls, li + 1);
      if (u == 10) { WAITPF(10); post(rWS, WS_F3 + (unsigned)(((rb * 8 + w) * 2) * 8 + j) * 128u, tag, lane); }
      SBAR();
    }
    STAMP(13);
    epi(2); epi(3);
    __builtin_amdgcn_s_setprio(0);
    SBAR();
    ldw2(6);
    SBAR();
    xfw2(1); xfw2(2);
    SBAR();
    WAITPF(4);
    post(rWS, WS_F3 + (unsigned)(((rb * 8 + w) * 2 + 1) * 8 + j) * 128u, tag, lane);
    STAMP(14);
  }

  {
    const unsigned f3 = (lane < 7 && lane != j) ? WS_F3 + (unsigned)(((rb * 8 + w) * 2) * 8 + lane) * 128u : 0u;
    poll(ws, f3, tag);
    STAMP(15);
    v4u fr[4][6];
    auto ldf = [&](int m, int s) {
      int sg = j + s; sg = sg >= 7 ? sg - 7 : sg;
      fr[m][s - 1] = ldcs(rWS, lane * 16u + m * 1024u, WS_F + (unsigned)(rb * 56 + 8 * sg + w) * 4096u);
      SBAR();
    };
#pragma unroll
    for (int s = 1; s < 7; ++s) ldf(0, s);
    ldf(1, 1); ldf(1, 2);
    f32x4 a[4][4];
#pragma unroll
    for (int m = 0; m < 4; ++m)
#pragma unroll
      for (int n = 0; n < 4; ++n) a[m][n] = mfma(ff[m], w2r[0][n], f32x4{0, 0, 0, 0});
    xfw2(3); xfw2(4); xfw2(5); xfw2(6);
    SBAR();
    BAR();
    STAMP(20);
    __builtin_amdgcn_s_setprio(2);
    int rw = L_R + w * 16384 + 4 * g * 256 + tok * 16;
    PIN(rw);
    unsigned long long fb = 0;
#pragma unroll
    for (int q = 0; q < 24; ++q) {
      const int m = q / 6, s = 1 + q % 6, qn = q + 8;
      if (q == 0) fb = __hip_atomic_load((gu64*)(ws + (f3 ? f3 + 1024u : 0u)), RLX_AGENT);
      if (q == 4) { if (!__all(f3 == 0 || fb == tag)) poll(ws, f3 ? f3 + 1024u : 0u, tag); STAMP(21); }
      if (qn < 24) ldf(qn / 6, 1 + qn % 6);
#pragma unroll
      for (int n = 0; n < 4; ++n) a[m][n] = mfma(fr[m][s - 1], w2r[s][n], a[m][n]);
      if (m > 0 && s <= 4) {
        const int r = s - 1, mp = m - 1;
        *(LAS f32x4*)(L + ((rw ^ ((r & 2) << 6)) + (16 * mp + r) * 256)) = f32x4{a[mp][0][r], a[mp][1][r], a[mp][2][r], a[mp][3][r]};
      }
      SBAR();
    }
#pragma unroll
    for (int r = 0; r < 4; ++r)
      *(LAS f32x4*)(L + ((rw ^ ((r & 2) << 6)) + (48 + r) * 256)) = f32x4{a[3][0][r], a[3][1][r], a[3][2][r], a[3][3][r]};
    STAMP(16);
    LBAR();
    STAMP(17);
    const int ra = L_R + row8 * 256 + ((c8 ^ ((row8 & 2) << 2)) * 16);
    f32x4 y0 = {0, 0, 0, 0}, y1 = {0, 0, 0, 0};
#pragma unroll
    for (int p = 0; p < 8; ++p) { y0 += *(LAS f32x4*)(L + ra + p * 16384); y1 += *(LAS f32x4*)(L + (ra ^ 128) + p * 16384); }
    const v4u hv = *(LAS v4u*)(L + L_HS + row8 * 144 + c8 * 16);
    const v4u ov = {pk(blo(hv.x) + y0[0], bhi(hv.x) + y0[1]), pk(blo(hv.y) + y0[2], bhi(hv.y) + y0[3]),
                    pk(blo(hv.z) + y1[0], bhi(hv.z) + y1[1]), pk(blo(hv.w) + y1[2], bhi(hv.w) + y1[3])};
    __builtin_amdgcn_raw_buffer_store_b128(ov, rOUT, (unsigned)(rb * 64 + row8) * 896u + j * 128u + c8 * 16u, 0, 0);
    STAMP(18);
  }
  asm volatile(".Lfb_end:" ::: "memory");
}
}

extern "C" void kernel_launch(void* const* d_in, const int* in_sizes, int n_in, void* d_out, int out_size, void* d_ws, size_t ws_size,
                              hipStream_t stream) {
  Args A{(const unsigned char*)d_in[0], (const unsigned char*)d_in[1], (const unsigned char*)d_in[2], (const unsigned char*)d_in[4],
         (const unsigned char*)d_in[5], (unsigned char*)d_out, (unsigned char*)d_ws};
  fused_block<<<256, 512, 0, stream>>>(A);
}
```
